```python
import torch

def _softmax(x, axis=-1):
    return torch.softmax(x, dim=axis)
def _gelu(x):
    return torch.nn.functional.gelu(x, approximate="tanh")
def _silu(x):
    return torch.nn.functional.silu(x)
def _sigmoid(x):
    return torch.sigmoid(x)
def _layernorm(x, axis=-1, eps=1e-5):
    m = x.mean(dim=axis, keepdim=True)
    v = x.var(dim=axis, keepdim=True, unbiased=False)
    return (x - m) / torch.sqrt(v + eps)
def _scatter_add(tgt, idx, src, axis=0):
    return tgt.clone().index_add_(axis, idx, src)
def _segment_sum(src, seg, n):
    out = torch.zeros((n,) + src.shape[1:], dtype=src.dtype)
    return out.index_add_(0, seg, src)
def _pad(x, pw):
    flat = tuple(v for pair in reversed(pw) for v in pair)
    return torch.nn.functional.pad(x, flat)

kernel_name = 'parallel_sum__sliding_block_cached__dense_activation_silu__decode'

def setup_inputs():

    _w = (lambda a: (lambda t: t.to(torch.bfloat16) if t.is_floating_point() else t)(torch.as_tensor(a)))
    def _A_setup():
        B, Sq, D = 1, 1, 640
        Hq, Hkv, Sk, Dh = 32, 32, 4096, 128
        assert Hq % Hkv == 0, (Hq, Hkv)
        return {
            "x": _w((lambda *s: torch.randn(*s, dtype=torch.bfloat16) * 0.05)(B, Sq, D)),
            "k_cache": _w((lambda *s: torch.randn(*s, dtype=torch.bfloat16) * 3.2)(B, Hkv, Sk, Dh)),
            "v_cache": _w((lambda *s: torch.randn(*s, dtype=torch.bfloat16) * 0.8)(B, Hkv, Sk, Dh)),
            "wq": _w((lambda *s: torch.randn(*s, dtype=torch.bfloat16) * 3.2)(D, Hq * Dh) * (D ** -0.5)),
            "wo": _w((lambda *s: torch.randn(*s, dtype=torch.bfloat16) * 0.8)(Hq * Dh, D) * ((Hq * Dh) ** -0.5)),
        }
    _A_in = _A_setup()

    def _B_setup():
        B, S, D, F = 1, 1, 640, 1728
        return {
            "x": _w((lambda *s: torch.randn(*s, dtype=torch.bfloat16) * 0.05)(B, S, D)),
            "w1": _w((lambda *s: torch.randn(*s, dtype=torch.bfloat16) * 3.2)(D, F) * (D ** -0.5)),
            "w2": _w((lambda *s: torch.randn(*s, dtype=torch.bfloat16) * 0.4)(F, D) * (F ** -0.5)),
        }
    _B_in = _B_setup()

    return {
        "A_x": _A_in["x"],
        "A_k_cache": _A_in["k_cache"],
        "A_v_cache": _A_in["v_cache"],
        "A_wq": _A_in["wq"],
        "A_wo": _A_in["wo"],
        "B_x": _B_in["x"],
        "B_w1": _B_in["w1"],
        "B_w2": _B_in["w2"],
    }

def reference(A_x, A_k_cache, A_v_cache, A_wq, A_wo, B_x, B_w1, B_w2):
    def _A_ref(x, k_cache, v_cache, wq, wo):
        B, Sq, D = x.shape
        Hq, Dh = 32, 128
        Hkv, Sk = k_cache.shape[1], k_cache.shape[2]
        W = min(4096, max(256, Sk // 4))
        rep = Hq // Hkv
        q = (lambda x, *d: x.permute(*d))((x @ wq).reshape(B, Sq, Hq, Dh), 0, 2, 1, 3)
        k = (lambda x, n, axis: torch.repeat_interleave(x, n, dim=axis))(k_cache[:, :, Sk - W:, :], rep, axis=1)
        v = (lambda x, n, axis: torch.repeat_interleave(x, n, dim=axis))(v_cache[:, :, Sk - W:, :], rep, axis=1)
        scores = torch.einsum("bhqd,bhkd->bhqk", q, k) * (Dh ** -0.5)
        p = _softmax(scores, axis=-1)
        o = torch.einsum("bhqk,bhkd->bhqd", p, v)
        return (lambda x, *d: x.permute(*d))(o, 0, 2, 1, 3).reshape(B, Sq, Hq * Dh) @ wo

    def _B_ref(x, w1, w2):
        h = torch.einsum("bsd,df->bsf", x, w1)
        h = _silu(h)
        out = torch.einsum("bsf,fd->bsd", h, w2)
        return out

    x = A_x
    xn = _layernorm(x, axis=-1)
    return x + _A_ref(xn, A_k_cache, A_v_cache, A_wq, A_wo) + _B_ref(xn, B_w1, B_w2)
```

```cpp
#include <hip/hip_runtime.h>
#include <hip/hip_bf16.h>
#include <stdint.h>

namespace {

constexpr int D = 640, NH = 32, DH = 128, SK = 4096, WIN = 1024, FF = 1728;
constexpr int NWG = 256, NTHR = 512;

typedef unsigned v4u __attribute__((ext_vector_type(4)));
typedef unsigned v2u __attribute__((ext_vector_type(2)));
typedef float f32x4 __attribute__((ext_vector_type(4)));
typedef float f32x2 __attribute__((ext_vector_type(2)));
typedef short bf16x8 __attribute__((ext_vector_type(8)));
typedef __bf16 bf16x2_t __attribute__((ext_vector_type(2)));
#define LAS __attribute__((address_space(3)))

constexpr int WS_EPOCH = 0;
constexpr int WS_H2 = 4096;
constexpr int WS_H3 = WS_H2 + 256 * 1024;
constexpr int WS_END = WS_H3 + 8 * 20 * 32 * 16;

constexpr int L_XD = 0;
constexpr int L_QS = 2560;
constexpr int L_QP = L_QS + 4096;
constexpr int L_MX = L_QP + 256;
constexpr int L_SL = L_MX + 64;
constexpr int L_OS = L_SL + 8192;
constexpr int L_END = L_OS + 4096;

constexpr int AUX_SC1 = 16;
constexpr int AUX_NT = 2;
#define KV_AUX 0
#define W_AUX 0
constexpr unsigned SPIN_CAP = 1u << 18;

__device__ __forceinline__ __amdgpu_buffer_rsrc_t mk_rsrc(const void* p, int bytes) {
    return __builtin_amdgcn_make_buffer_rsrc((void*)p, 0, bytes, 0x00020000);
}
template <int AUX> __device__ __forceinline__ v4u ld128(__amdgpu_buffer_rsrc_t r, int voff, int soff) {
    return __builtin_amdgcn_raw_buffer_load_b128(r, voff, soff, AUX);
}
__device__ __forceinline__ float u2f(unsigned u) { return __builtin_bit_cast(float, u); }
__device__ __forceinline__ unsigned f2u(float f) { return __builtin_bit_cast(unsigned, f); }
__device__ __forceinline__ float bflo(unsigned u) { return u2f(u << 16); }
__device__ __forceinline__ float bfhi(unsigned u) { return u2f(u & 0xffff0000u); }
__device__ __forceinline__ unsigned cvtpk(float lo, float hi) {
    bf16x2_t t = {(__bf16)lo, (__bf16)hi};
    return __builtin_bit_cast(unsigned, t);
}
__device__ __forceinline__ float rbf(float x) { return bflo(cvtpk(x, x) & 0xffffu); }
template <int CTRL> __device__ __forceinline__ float dpp(float x) {
    return u2f(__builtin_amdgcn_mov_dpp(f2u(x), CTRL, 0xf, 0xf, true));
}
constexpr int XOR1 = 0xB1, XOR2 = 0x4E, HMIR = 0x141, ROR8 = 0x128, ROR4 = 0x124, ROR2 = 0x122, ROR1 = 0x121;
__device__ __forceinline__ float row16_sum(float x) {
    x += dpp<ROR8>(x); x += dpp<ROR4>(x); x += dpp<ROR2>(x); x += dpp<ROR1>(x); return x;
}
__device__ __forceinline__ float oct_sum(float x) {
    x += dpp<XOR1>(x); x += dpp<XOR2>(x); x += dpp<HMIR>(x); return x;
}
__device__ __forceinline__ float oct_max(float x) {
    x = fmaxf(x, dpp<XOR1>(x)); x = fmaxf(x, dpp<XOR2>(x)); x = fmaxf(x, dpp<HMIR>(x)); return x;
}
__device__ __forceinline__ float x16_sum(float x) {
    auto s = __builtin_amdgcn_permlane16_swap(f2u(x), f2u(x), false, false);
    return u2f(s[0]) + u2f(s[1]);
}
__device__ __forceinline__ float x32_sum(float x) {
    auto s = __builtin_amdgcn_permlane32_swap(f2u(x), f2u(x), false, false);
    return u2f(s[0]) + u2f(s[1]);
}
__device__ __forceinline__ float x16_max(float x) {
    auto s = __builtin_amdgcn_permlane16_swap(f2u(x), f2u(x), false, false);
    return fmaxf(u2f(s[0]), u2f(s[1]));
}
__device__ __forceinline__ float x32_max(float x) {
    auto s = __builtin_amdgcn_permlane32_swap(f2u(x), f2u(x), false, false);
    return fmaxf(u2f(s[0]), u2f(s[1]));
}
__device__ __forceinline__ float dot2(unsigned a, unsigned b, float c) {
    return __builtin_amdgcn_fdot2_f32_bf16(__builtin_bit_cast(bf16x2_t, a), __builtin_bit_cast(bf16x2_t, b), c, false);
}
typedef __bf16 bf16x8_t __attribute__((ext_vector_type(8)));
__device__ __forceinline__ f32x4 vmm(unsigned dup, const unsigned (&mk)[4], v4u w, f32x4 acc) {
    v4u a = {dup & mk[0], dup & mk[1], dup & mk[2], dup & mk[3]};
    return __builtin_amdgcn_mfma_f32_16x16x32_bf16(__builtin_bit_cast(bf16x8_t, a), __builtin_bit_cast(bf16x8_t, w), acc, 0, 0, 0);
}
__device__ __forceinline__ v4u tag4(f32x4 v, unsigned tag) {
    v4u o;
    o.x = (f2u(v.x) & 0xffffff00u) | tag; o.y = (f2u(v.y) & 0xffffff00u) | tag;
    o.z = (f2u(v.z) & 0xffffff00u) | tag; o.w = (f2u(v.w) & 0xffffff00u) | tag;
    return o;
}
__device__ __forceinline__ bool tag_ok(v4u g, unsigned tag) {
    return (((g.x ^ tag) | (g.y ^ tag) | (g.z ^ tag) | (g.w ^ tag)) & 0xffu) == 0u;
}
__device__ __forceinline__ f32x4 untag(v4u g) {
    return f32x4{u2f(g.x & 0xffffff00u), u2f(g.y & 0xffffff00u), u2f(g.z & 0xffffff00u), u2f(g.w & 0xffffff00u)};
}

__global__ void __launch_bounds__(NTHR, 2)
fused_decode_kernel(const void* __restrict__ px, const void* __restrict__ pk, const void* __restrict__ pv,
                    const void* __restrict__ pwq, const void* __restrict__ pwo, const void* __restrict__ pw1,
                    const void* __restrict__ pw2, void* __restrict__ pout, void* __restrict__ pws) {
    __shared__ __attribute__((aligned(16))) unsigned char smem[L_END];
    LAS unsigned char* L = (LAS unsigned char*)smem;
    const int b = blockIdx.x, idx = b >> 3, head = (b & 7) + 8 * (idx >> 3), c = idx & 7;
    const int tid = threadIdx.x, l = tid & 63, w = __builtin_amdgcn_readfirstlane(tid >> 6), g4 = l >> 4, cc = l & 15;

    const __amdgpu_buffer_rsrc_t rX = mk_rsrc(px, D * 2), rK = mk_rsrc(pk, NH * SK * DH * 2), rV = mk_rsrc(pv, NH * SK * DH * 2),
                                 rWQ = mk_rsrc(pwq, D * NH * DH * 2), rWO = mk_rsrc(pwo, NH * DH * D * 2),
                                 rW1 = mk_rsrc(pw1, D * FF * 2), rW2 = mk_rsrc(pw2, FF * D * 2),
                                 rWS = mk_rsrc(pws, WS_END), rO = mk_rsrc(pout, D * 2);

    const v4u xa = ld128<0>(rX, l * 20, 0);
    const unsigned xb = __builtin_amdgcn_raw_buffer_load_b32(rX, l * 20 + 16, 0, 0);
    const unsigned epoch = __builtin_amdgcn_raw_buffer_load_b32(rWS, WS_EPOCH, 0, AUX_SC1);
    const v2u xres = __builtin_amdgcn_raw_buffer_load_b64(rX, (80 * c + 8 * (head < 10 ? head : 0) + 4 * (l >> 5)) * 2, 0, 0);
    __builtin_amdgcn_sched_barrier(0);
    const int k0 = (5 * c) >> 3;
    v4u wq[20];
    {
        const int voff = (80 * w + 20 * g4) * 8192 + head * 256 + cc * 16;
#pragma unroll
        for (int k = 0; k < 5; ++k) {
            int kk = k0 + k; kk = kk >= 5 ? kk - 5 : kk;
#pragma unroll
            for (int j = 0; j < 4; ++j) wq[4 * k + j] = ld128<0>(rWQ, voff, (4 * kk + j) * 8192);
        }
    }
    __builtin_amdgcn_sched_barrier(0);
    v4u w1a, w1b;
    {
        const int voff = head < 27 ? (80 * c + 8 * w + 2 * g4 + (cc >> 3)) * (FF * 2) + head * 128 + (cc & 7) * 16 : 0x7ffffff0;
        w1a = ld128<W_AUX>(rW1, voff, 0);
        w1b = ld128<W_AUX>(rW1, (w < 2) ? voff : 0x7ffffff0, (w < 2) ? 64 * FF * 2 : 0);
    }
    v4u kt[4], vt[4];
    const int kvoff = (head * SK + (SK - WIN) + c * 128 + w * 16 + g4) * 256 + cc * 16;
#pragma unroll
    for (int t = 0; t < 4; ++t) kt[t] = ld128<KV_AUX>(rK, kvoff + t * 1024, 0);
    __builtin_amdgcn_sched_barrier(0);
#pragma unroll
    for (int t = 0; t < 4; ++t) vt[t] = ld128<KV_AUX>(rV, kvoff + t * 1024, 0);
    __builtin_amdgcn_sched_barrier(0);
    v4u wo[4], w2[2];
    {
        const int ccl = cc < 10 ? cc : 9;
        const int voff = (head * 128 + 16 * w + 4 * g4) * 1280 + c * 160 + ccl * 16;
#pragma unroll
        for (int i = 0; i < 4; ++i) wo[i] = ld128<W_AUX>(rWO, voff + i * 1280, 0);
        const int voff2 = (head * 64 + 8 * w + 2 * g4) * 1280 + c * 160 + ccl * 16;
#pragma unroll
        for (int i = 0; i < 2; ++i) w2[i] = ld128<W_AUX>(rW2, voff2 + i * 1280, 0);
    }
    __builtin_amdgcn_sched_barrier(0);

    unsigned mk[4];
#pragma unroll
    for (int r = 0; r < 4; ++r) mk[r] = ((cc & 7) >> 1) == r ? ((cc & 1) ? 0xffff0000u : 0x0000ffffu) : 0u;

    {
        float xv[10] = {bflo(xa.x), bfhi(xa.x), bflo(xa.y), bfhi(xa.y), bflo(xa.z), bfhi(xa.z), bflo(xa.w), bfhi(xa.w), bflo(xb), bfhi(xb)};
        float s = 0.f;
#pragma unroll
        for (int i = 0; i < 10; ++i) s += xv[i];
        s = x32_sum(x16_sum(row16_sum(s)));
        const float mean = s * (1.0f / D);
        float q2 = 0.f;
#pragma unroll
        for (int i = 0; i < 10; ++i) { const float d = xv[i] - mean; q2 = fmaf(d, d, q2); }
        q2 = x32_sum(x16_sum(row16_sum(q2)));
        const float var = rbf(q2 * (1.0f / D));
        const float sd = rbf(__builtin_amdgcn_sqrtf(rbf(var + 1e-5f)));
        const float rinv = __builtin_amdgcn_rcpf(sd), mb = rbf(mean);
        unsigned dup[10];
#pragma unroll
        for (int i = 0; i < 10; ++i) { const float y = rbf(xv[i] - mb) * rinv; dup[i] = cvtpk(y, y); }
        LAS v2u* xd = (LAS v2u*)(L + L_XD + l * 40);
#pragma unroll
        for (int i = 0; i < 5; ++i) xd[i] = v2u{dup[2 * i], dup[2 * i + 1]};
    }
    __builtin_amdgcn_fence(__ATOMIC_RELEASE, "wavefront");
    asm volatile("s_waitcnt lgkmcnt(0)" ::: "memory");

    {
        f32x4 acc0 = {0.f, 0.f, 0.f, 0.f}, acc1 = acc0;
#pragma unroll
        for (int k = 0; k < 5; ++k) {
            int kk = k0 + k; kk = kk >= 5 ? kk - 5 : kk;
            const v4u dd = *(LAS v4u*)(L + L_XD + (80 * w + 20 * g4 + 4 * kk) * 4);
            acc0 = vmm(dd.x, mk, wq[4 * k + 0], acc0);
            acc1 = vmm(dd.y, mk, wq[4 * k + 1], acc1);
            acc0 = vmm(dd.z, mk, wq[4 * k + 2], acc0);
            acc1 = vmm(dd.w, mk, wq[4 * k + 3], acc1);
        }
        acc0 += acc1;
        if (g4 < 2) *(LAS f32x4*)(L + L_QS + w * 512 + (8 * cc + 4 * g4) * 4) = acc0;
    }
    {
        const unsigned da = *(LAS unsigned*)(L + L_XD + (80 * c + 8 * w + 2 * g4 + (cc >> 3)) * 4);
        const unsigned db = *(LAS unsigned*)(L + L_XD + (80 * c + 64 + (w < 2 ? 8 * w : 0) + 2 * g4 + (cc >> 3)) * 4);
        f32x4 h = {0.f, 0.f, 0.f, 0.f};
        h = vmm(da, mk, w1a, h);
        h = vmm(db, mk, w1b, h);
        f32x4 hs;
#pragma unroll
        for (int r = 0; r < 4; ++r) {
            auto s = __builtin_amdgcn_permlane32_swap(f2u(h[r]), f2u(h[r]), false, false);
            hs[r] = h[r] + dpp<ROR8>(u2f(s[1]));
        }
        if (g4 < 2 && cc < 8) {
            LAS unsigned char* p = L + L_SL + w * 1024 + (cc * 8 + 4 * g4 + 1) * 16 + 8;
            *(LAS f32x2*)(p) = f32x2{hs[0], hs[1]};
            *(LAS f32x2*)(p + 32) = f32x2{hs[2], hs[3]};
        }
    }
    __syncthreads();
    unsigned qp[4];
    {
        f32x2 a = *(LAS f32x2*)(L + L_QS + l * 8);
#pragma unroll
        for (int ww = 1; ww < 8; ++ww) a += *(LAS f32x2*)(L + L_QS + ww * 512 + l * 8);
        *(LAS unsigned*)(L + L_QP + l * 4) = cvtpk(a.x, a.y);
        asm volatile("s_waitcnt lgkmcnt(0)" ::: "memory");
        const v4u q4 = *(LAS v4u*)(L + L_QP + cc * 16);
        qp[0] = q4.x; qp[1] = q4.y; qp[2] = q4.z; qp[3] = q4.w;
    }
    float sc[4];
#pragma unroll
    for (int t = 0; t < 4; ++t) {
        float s = dot2(kt[t].x, qp[0], 0.f);
        s = dot2(kt[t].y, qp[1], s); s = dot2(kt[t].z, qp[2], s); s = dot2(kt[t].w, qp[3], s);
        s = row16_sum(s);
        sc[t] = rbf(rbf(s) * 0.08838834764831845f);
    }
    float m = fmaxf(fmaxf(sc[0], sc[1]), fmaxf(sc[2], sc[3]));
    m = x32_max(x16_max(m));
    if (l == 0) *(LAS float*)(L + L_MX + w * 4) = m;
    __syncthreads();
    {
        const f32x4 a = *(LAS f32x4*)(L + L_MX), bb = *(LAS f32x4*)(L + L_MX + 16);
        m = fmaxf(fmaxf(fmaxf(a.x, a.y), fmaxf(a.z, a.w)), fmaxf(fmaxf(bb.x, bb.y), fmaxf(bb.z, bb.w)));
    }
    unsigned pd[4];
    float lsum = 0.f;
#pragma unroll
    for (int t = 0; t < 4; ++t) {
        const float p = __builtin_amdgcn_exp2f((sc[t] - m) * 1.4426950408889634f);
        lsum += p;
        pd[t] = cvtpk(p, p);
    }
    lsum = x32_sum(x16_sum(lsum));
    if (l < 32) *(LAS f32x2*)(L + L_SL + w * 1024 + (2 * l + 1) * 16) = f32x2{w == 0 ? m : 0.f, lsum};
    {
        f32x4 o = {0.f, 0.f, 0.f, 0.f};
#pragma unroll
        for (int t = 0; t < 4; ++t) o = vmm(pd[t], mk, vt[t], o);
        if (g4 < 2) { const int gam = 2 * cc + g4; *(LAS f32x4*)(L + L_SL + w * 1024 + (2 * gam) * 16) = o; }
    }
    __syncthreads();
    const unsigned tag = (epoch + 1u) & 0xffu;
    if (w == 0) {
        f32x4 s = *(LAS f32x4*)(L + L_SL + l * 16);
#pragma unroll
        for (int ww = 1; ww < 8; ++ww) s += *(LAS f32x4*)(L + L_SL + ww * 1024 + l * 16);
        __builtin_amdgcn_raw_buffer_store_b128(tag4(s, tag), rWS, WS_H2 + (head * 8 + c) * 1024 + l * 16, 0, AUX_SC1);
    }
    v4u g;
    {
        const int off = WS_H2 + (head * 8 + (cc & 7)) * 1024 + (w * 8 + g4 * 2 + (cc >> 3)) * 16;
        for (unsigned spins = 0;; ++spins) {
            asm volatile("" ::: "memory");
            g = ld128<AUX_SC1>(rWS, off, 0);
            if (__all(tag_ok(g, tag)) || spins > SPIN_CAP) break;
            __builtin_amdgcn_s_sleep(1);
        }
    }
    {
        f32x4 v = untag(g);
        const bool lo = cc < 8;
        const float om = dpp<ROR8>(v.x), ol = dpp<ROR8>(v.y);
        const float Mc = lo ? om : v.x, Lc = lo ? ol : v.y;
        const float M = oct_max(Mc);
        const float wt = __builtin_amdgcn_exp2f((Mc - M) * 1.4426950408889634f);
        const float Lt = oct_sum(wt * Lc);
        const float f = lo ? wt : 1.0f;
        f32x4 t;
#pragma unroll
        for (int i = 0; i < 4; ++i) t[i] = oct_sum(v[i] * f);
        const float inv = __builtin_amdgcn_rcpf(Lt);
        f32x4 a1 = {0.f, 0.f, 0.f, 0.f}, a2 = a1;
        unsigned hd[2];
#pragma unroll
        for (int i = 0; i < 2; ++i) {
            const float hh = rbf(t[2 + i]);
            const float sl = hh * __builtin_amdgcn_rcpf(1.0f + __builtin_amdgcn_exp2f(-hh * 1.4426950408889634f));
            hd[i] = cvtpk(sl, sl);
        }
#pragma unroll
        for (int i = 0; i < 4; ++i) { const float ov = t[i] * inv; a1 = vmm(cvtpk(ov, ov), mk, wo[i], a1); }
#pragma unroll
        for (int i = 0; i < 2; ++i) a2 = vmm(hd[i], mk, w2[i], a2);
        f32x4 sel = g4 < 2 ? a1 : a2, tot;
#pragma unroll
        for (int r = 0; r < 4; ++r) tot[r] = x32_sum(sel[r]);
        if (g4 < 2 && cc < 10) *(LAS f32x4*)(L + L_OS + w * 512 + (2 * cc + g4) * 16) = tot;
    }
    __syncthreads();
    if (w == 0) {
        const int gam = l < 20 ? l : 0;
        f32x4 s = *(LAS f32x4*)(L + L_OS + gam * 16);
#pragma unroll
        for (int ww = 1; ww < 8; ++ww) s += *(LAS f32x4*)(L + L_OS + ww * 512 + gam * 16);
        if (l < 20) __builtin_amdgcn_raw_buffer_store_b128(tag4(s, tag), rWS, WS_H3 + ((c * 20 + l) * 32 + head) * 16, 0, AUX_SC1);
        if (head < 10) {
            const int off = WS_H3 + (c * 20 + 2 * head) * 512 + l * 16;
            v4u g3;
            for (unsigned spins = 0;; ++spins) {
                asm volatile("" ::: "memory");
                g3 = ld128<AUX_SC1>(rWS, off, 0);
                if (__all(tag_ok(g3, tag)) || spins > SPIN_CAP) break;
                __builtin_amdgcn_s_sleep(1);
            }
            f32x4 v = untag(g3);
#pragma unroll
            for (int i = 0; i < 4; ++i) v[i] = x16_sum(row16_sum(v[i]));
            v2u o;
            o.x = cvtpk(v.x + bflo(xres.x), v.y + bfhi(xres.x));
            o.y = cvtpk(v.z + bflo(xres.y), v.w + bfhi(xres.y));
            if ((l & 31) == 0) __builtin_amdgcn_raw_buffer_store_b64(o, rO, (80 * c + 8 * head + 4 * (l >> 5)) * 2, 0, 0);
            if (b == 0 && l == 0) __builtin_amdgcn_raw_buffer_store_b32(epoch + 1u, rWS, WS_EPOCH, 0, AUX_SC1);
        }
    }
}

}

extern "C" void kernel_launch(void* const* d_in, const int* in_sizes, int n_in, void* d_out, int out_size, void* d_ws,
                              size_t ws_size, hipStream_t stream) {
    fused_decode_kernel<<<NWG, NTHR, 0, stream>>>(d_in[0], d_in[1], d_in[2], d_in[3], d_in[4], d_in[6], d_in[7], d_out, d_ws);
}
```

```cpp
#include <hip/hip_runtime.h>
#include <hip/hip_bf16.h>
#include <stdint.h>

namespace {

constexpr int D = 640, NH = 32, DH = 128, SK = 4096, WIN = 1024, FF = 1728;
constexpr int NWG = 256, NTHR = 512;

typedef unsigned v4u __attribute__((ext_vector_type(4)));
typedef unsigned v2u __attribute__((ext_vector_type(2)));
typedef float f32x4 __attribute__((ext_vector_type(4)));
typedef float f32x2 __attribute__((ext_vector_type(2)));
typedef short bf16x8 __attribute__((ext_vector_type(8)));
typedef __bf16 bf16x2_t __attribute__((ext_vector_type(2)));
#define LAS __attribute__((address_space(3)))

constexpr int WS_EPOCH = 0;
constexpr int WS_H2 = 4096;
constexpr int WS_H3 = WS_H2 + 256 * 1024;
constexpr int WS_END = WS_H3 + 8 * 20 * 32 * 16;

constexpr int L_XD = 0;
constexpr int L_QS = 2560;
constexpr int L_QP = L_QS + 4096;
constexpr int L_MX = L_QP + 256;
constexpr int L_SL = L_MX + 64;
constexpr int L_OS = L_SL + 8192;
constexpr int L_END = L_OS + 4096;

constexpr int AUX_SC1 = 16;
constexpr int AUX_NT = 2;
#define KV_AUX 0
#define W_AUX 0
constexpr unsigned SPIN_CAP = 1u << 18;

__device__ __forceinline__ __amdgpu_buffer_rsrc_t mk_rsrc(const void* p, int bytes) {
    return __builtin_amdgcn_make_buffer_rsrc((void*)p, 0, bytes, 0x00020000);
}
template <int AUX> __device__ __forceinline__ v4u ld128(__amdgpu_buffer_rsrc_t r, int voff, int soff) {
    return __builtin_amdgcn_raw_buffer_load_b128(r, voff, soff, AUX);
}
__device__ __forceinline__ float u2f(unsigned u) { return __builtin_bit_cast(float, u); }
__device__ __forceinline__ unsigned f2u(float f) { return __builtin_bit_cast(unsigned, f); }
__device__ __forceinline__ float bflo(unsigned u) { return u2f(u << 16); }
__device__ __forceinline__ float bfhi(unsigned u) { return u2f(u & 0xffff0000u); }
__device__ __forceinline__ unsigned cvtpk(float lo, float hi) {
    bf16x2_t t = {(__bf16)lo, (__bf16)hi};
    return __builtin_bit_cast(unsigned, t);
}
__device__ __forceinline__ float rbf(float x) { return bflo(cvtpk(x, x) & 0xffffu); }
template <int CTRL> __device__ __forceinline__ float dpp(float x) {
    return u2f(__builtin_amdgcn_mov_dpp(f2u(x), CTRL, 0xf, 0xf, true));
}
constexpr int XOR1 = 0xB1, XOR2 = 0x4E, HMIR = 0x141, ROR8 = 0x128, ROR4 = 0x124, ROR2 = 0x122, ROR1 = 0x121;
__device__ __forceinline__ float row16_sum(float x) {
    x += dpp<ROR8>(x); x += dpp<ROR4>(x); x += dpp<ROR2>(x); x += dpp<ROR1>(x); return x;
}
__device__ __forceinline__ float oct_sum(float x) {
    x += dpp<XOR1>(x); x += dpp<XOR2>(x); x += dpp<HMIR>(x); return x;
}
__device__ __forceinline__ float oct_max(float x) {
    x = fmaxf(x, dpp<XOR1>(x)); x = fmaxf(x, dpp<XOR2>(x)); x = fmaxf(x, dpp<HMIR>(x)); return x;
}
__device__ __forceinline__ float x16_sum(float x) {
    auto s = __builtin_amdgcn_permlane16_swap(f2u(x), f2u(x), false, false);
    return u2f(s[0]) + u2f(s[1]);
}
__device__ __forceinline__ float x32_sum(float x) {
    auto s = __builtin_amdgcn_permlane32_swap(f2u(x), f2u(x), false, false);
    return u2f(s[0]) + u2f(s[1]);
}
__device__ __forceinline__ float x16_max(float x) {
    auto s = __builtin_amdgcn_permlane16_swap(f2u(x), f2u(x), false, false);
    return fmaxf(u2f(s[0]), u2f(s[1]));
}
__device__ __forceinline__ float x32_max(float x) {
    auto s = __builtin_amdgcn_permlane32_swap(f2u(x), f2u(x), false, false);
    return fmaxf(u2f(s[0]), u2f(s[1]));
}
__device__ __forceinline__ float dot2(unsigned a, unsigned b, float c) {
    return __builtin_amdgcn_fdot2_f32_bf16(__builtin_bit_cast(bf16x2_t, a), __builtin_bit_cast(bf16x2_t, b), c, false);
}
typedef __bf16 bf16x8_t __attribute__((ext_vector_type(8)));
__device__ __forceinline__ f32x4 vmm(unsigned dup, const unsigned (&mk)[4], v4u w, f32x4 acc) {
    v4u a = {dup & mk[0], dup & mk[1], dup & mk[2], dup & mk[3]};
    return __builtin_amdgcn_mfma_f32_16x16x32_bf16(__builtin_bit_cast(bf16x8_t, a), __builtin_bit_cast(bf16x8_t, w), acc, 0, 0, 0);
}
__device__ __forceinline__ v4u tag4(f32x4 v, unsigned tag) {
    v4u o;
    o.x = (f2u(v.x) & 0xffffff00u) | tag; o.y = (f2u(v.y) & 0xffffff00u) | tag;
    o.z = (f2u(v.z) & 0xffffff00u) | tag; o.w = (f2u(v.w) & 0xffffff00u) | tag;
    return o;
}
__device__ __forceinline__ bool tag_ok(v4u g, unsigned tag) {
    return (((g.x ^ tag) | (g.y ^ tag) | (g.z ^ tag) | (g.w ^ tag)) & 0xffu) == 0u;
}
__device__ __forceinline__ f32x4 untag(v4u g) {
    return f32x4{u2f(g.x & 0xffffff00u), u2f(g.y & 0xffffff00u), u2f(g.z & 0xffffff00u), u2f(g.w & 0xffffff00u)};
}

#define STAMP(i)
#define STAMP_DECL
#define STAMP_DUMP

__global__ void __launch_bounds__(NTHR, 2)
fused_decode_kernel(const void* __restrict__ px, const void* __restrict__ pk, const void* __restrict__ pv,
                    const void* __restrict__ pwq, const void* __restrict__ pwo, const void* __restrict__ pw1,
                    const void* __restrict__ pw2, void* __restrict__ pout, void* __restrict__ pws) {
    __shared__ __attribute__((aligned(16))) unsigned char smem[L_END];
    LAS unsigned char* L = (LAS unsigned char*)smem;
    const int b = blockIdx.x, idx = b >> 3, head = (b & 7) + 8 * (idx >> 3), c = idx & 7;
    const int tid = threadIdx.x, l = tid & 63, w = __builtin_amdgcn_readfirstlane(tid >> 6), g4 = l >> 4, cc = l & 15;

    const __amdgpu_buffer_rsrc_t rX = mk_rsrc(px, D * 2), rK = mk_rsrc(pk, NH * SK * DH * 2), rV = mk_rsrc(pv, NH * SK * DH * 2),
                                 rWQ = mk_rsrc(pwq, D * NH * DH * 2), rWO = mk_rsrc(pwo, NH * DH * D * 2),
                                 rW1 = mk_rsrc(pw1, D * FF * 2), rW2 = mk_rsrc(pw2, FF * D * 2),
                                 rWS = mk_rsrc(pws, WS_END), rO = mk_rsrc(pout, D * 2);

    STAMP_DECL; STAMP(0);
    const v4u xa = ld128<0>(rX, l * 20, 0);
    const unsigned xb = __builtin_amdgcn_raw_buffer_load_b32(rX, l * 20 + 16, 0, 0);
    const unsigned epoch = __builtin_amdgcn_raw_buffer_load_b32(rWS, WS_EPOCH, 0, AUX_SC1);
    const v2u xres = __builtin_amdgcn_raw_buffer_load_b64(rX, (80 * c + 8 * (head < 10 ? head : 0) + 4 * (l >> 5)) * 2, 0, 0);
    __builtin_amdgcn_sched_barrier(0);
    const int k0 = (5 * c) >> 3;
    v4u wq[20];
    {
        const int voff = (80 * w + 20 * g4) * 8192 + head * 256 + cc * 16;
#pragma unroll
        for (int k = 0; k < 5; ++k) {
            int kk = k0 + k; kk = kk >= 5 ? kk - 5 : kk;
#pragma unroll
            for (int j = 0; j < 4; ++j) wq[4 * k + j] = ld128<0>(rWQ, voff, (4 * kk + j) * 8192);
        }
    }
    __builtin_amdgcn_sched_barrier(0);
    v4u w1a, w1b;
    {
        const int voff = head < 27 ? (80 * c + 8 * w + 2 * g4 + (cc >> 3)) * (FF * 2) + head * 128 + (cc & 7) * 16 : 0x7ffffff0;
        w1a = ld128<W_AUX>(rW1, voff, 0);
        w1b = ld128<W_AUX>(rW1, (w < 2) ? voff : 0x7ffffff0, (w < 2) ? 64 * FF * 2 : 0);
    }
    v4u kt[4], vt[4];
    const int kvoff = (head * SK + (SK - WIN) + c * 128 + w * 16 + g4) * 256 + cc * 16;
#pragma unroll
    for (int t = 0; t < 4; ++t) kt[t] = ld128<KV_AUX>(rK, kvoff + t * 1024, 0);
    __builtin_amdgcn_sched_barrier(0);
#pragma unroll
    for (int t = 0; t < 4; ++t) vt[t] = ld128<KV_AUX>(rV, kvoff + t * 1024, 0);
    __builtin_amdgcn_sched_barrier(0);
    v4u wo[4], w2[2];
    {
        const int ccl = cc < 10 ? cc : 9;
        const int voff = (head * 128 + 16 * w + 4 * g4) * 1280 + c * 160 + ccl * 16;
#pragma unroll
        for (int i = 0; i < 4; ++i) wo[i] = ld128<W_AUX>(rWO, voff + i * 1280, 0);
        const int voff2 = (head * 64 + 8 * w + 2 * g4) * 1280 + c * 160 + ccl * 16;
#pragma unroll
        for (int i = 0; i < 2; ++i) w2[i] = ld128<W_AUX>(rW2, voff2 + i * 1280, 0);
    }
    __builtin_amdgcn_sched_barrier(0);

    STAMP(1);
    unsigned mk[4];
#pragma unroll
    for (int r = 0; r < 4; ++r) mk[r] = ((cc & 7) >> 1) == r ? ((cc & 1) ? 0xffff0000u : 0x0000ffffu) : 0u;

    {
        float xv[10] = {bflo(xa.x), bfhi(xa.x), bflo(xa.y), bfhi(xa.y), bflo(xa.z), bfhi(xa.z), bflo(xa.w), bfhi(xa.w), bflo(xb), bfhi(xb)};
        float s = 0.f;
#pragma unroll
        for (int i = 0; i < 10; ++i) s += xv[i];
        s = x32_sum(x16_sum(row16_sum(s)));
        const float mean = s * (1.0f / D);
        float q2 = 0.f;
#pragma unroll
        for (int i = 0; i < 10; ++i) { const float d = xv[i] - mean; q2 = fmaf(d, d, q2); }
        q2 = x32_sum(x16_sum(row16_sum(q2)));
        const float var = rbf(q2 * (1.0f / D));
        const float sd = rbf(__builtin_amdgcn_sqrtf(rbf(var + 1e-5f)));
        const float rinv = __builtin_amdgcn_rcpf(sd), mb = rbf(mean);
        unsigned dup[10];
#pragma unroll
        for (int i = 0; i < 10; ++i) { const float y = rbf(xv[i] - mb) * rinv; dup[i] = cvtpk(y, y); }
        LAS v2u* xd = (LAS v2u*)(L + L_XD + l * 40);
#pragma unroll
        for (int i = 0; i < 5; ++i) xd[i] = v2u{dup[2 * i], dup[2 * i + 1]};
    }
    __builtin_amdgcn_fence(__ATOMIC_RELEASE, "wavefront");
    asm volatile("s_waitcnt lgkmcnt(0)" ::: "memory");

    STAMP(2);
    {
        f32x4 acc0 = {0.f, 0.f, 0.f, 0.f}, acc1 = acc0;
#pragma unroll
        for (int k = 0; k < 5; ++k) {
            int kk = k0 + k; kk = kk >= 5 ? kk - 5 : kk;
            const v4u dd = *(LAS v4u*)(L + L_XD + (80 * w + 20 * g4 + 4 * kk) * 4);
            acc0 = vmm(dd.x, mk, wq[4 * k + 0], acc0);
            acc1 = vmm(dd.y, mk, wq[4 * k + 1], acc1);
            acc0 = vmm(dd.z, mk, wq[4 * k + 2], acc0);
            acc1 = vmm(dd.w, mk, wq[4 * k + 3], acc1);
        }
        acc0 += acc1;
        if (g4 < 2) *(LAS f32x4*)(L + L_QS + w * 512 + (8 * cc + 4 * g4) * 4) = acc0;
    }
    {
        const unsigned da = *(LAS unsigned*)(L + L_XD + (80 * c + 8 * w + 2 * g4 + (cc >> 3)) * 4);
        const unsigned db = *(LAS unsigned*)(L + L_XD + (80 * c + 64 + (w < 2 ? 8 * w : 0) + 2 * g4 + (cc >> 3)) * 4);
        f32x4 h = {0.f, 0.f, 0.f, 0.f};
        h = vmm(da, mk, w1a, h);
        h = vmm(db, mk, w1b, h);
        f32x4 hs;
#pragma unroll
        for (int r = 0; r < 4; ++r) {
            auto s = __builtin_amdgcn_permlane32_swap(f2u(h[r]), f2u(h[r]), false, false);
            hs[r] = h[r] + dpp<ROR8>(u2f(s[1]));
        }
        if (g4 < 2 && cc < 8) {
            LAS unsigned char* p = L + L_SL + w * 1024 + (cc * 8 + 4 * g4 + 1) * 16 + 8;
            *(LAS f32x2*)(p) = f32x2{hs[0], hs[1]};
            *(LAS f32x2*)(p + 32) = f32x2{hs[2], hs[3]};
        }
    }
    STAMP(3);
    __syncthreads();
    unsigned qp[4];
    {
        f32x2 a = *(LAS f32x2*)(L + L_QS + l * 8);
#pragma unroll
        for (int ww = 1; ww < 8; ++ww) a += *(LAS f32x2*)(L + L_QS + ww * 512 + l * 8);
        *(LAS unsigned*)(L + L_QP + l * 4) = cvtpk(a.x, a.y);
        asm volatile("s_waitcnt lgkmcnt(0)" ::: "memory");
        const v4u q4 = *(LAS v4u*)(L + L_QP + cc * 16);
        qp[0] = q4.x; qp[1] = q4.y; qp[2] = q4.z; qp[3] = q4.w;
    }
    STAMP(4);
    float sc[4];
#pragma unroll
    for (int t = 0; t < 4; ++t) {
        float s = dot2(kt[t].x, qp[0], 0.f);
        s = dot2(kt[t].y, qp[1], s); s = dot2(kt[t].z, qp[2], s); s = dot2(kt[t].w, qp[3], s);
        s = row16_sum(s);
        sc[t] = rbf(rbf(s) * 0.08838834764831845f);
    }
    float m = fmaxf(fmaxf(sc[0], sc[1]), fmaxf(sc[2], sc[3]));
    m = x32_max(x16_max(m));
    if (l == 0) *(LAS float*)(L + L_MX + w * 4) = m;
    STAMP(5);
    __syncthreads();
    {
        const f32x4 a = *(LAS f32x4*)(L + L_MX), bb = *(LAS f32x4*)(L + L_MX + 16);
        m = fmaxf(fmaxf(fmaxf(a.x, a.y), fmaxf(a.z, a.w)), fmaxf(fmaxf(bb.x, bb.y), fmaxf(bb.z, bb.w)));
    }
    unsigned pd[4];
    float lsum = 0.f;
#pragma unroll
    for (int t = 0; t < 4; ++t) {
        const float p = __builtin_amdgcn_exp2f((sc[t] - m) * 1.4426950408889634f);
        lsum += p;
        pd[t] = cvtpk(p, p);
    }
    lsum = x32_sum(x16_sum(lsum));
    if (l < 32) *(LAS f32x2*)(L + L_SL + w * 1024 + (2 * l + 1) * 16) = f32x2{w == 0 ? m : 0.f, lsum};
    {
        f32x4 o = {0.f, 0.f, 0.f, 0.f};
#pragma unroll
        for (int t = 0; t < 4; ++t) o = vmm(pd[t], mk, vt[t], o);
        if (g4 < 2) { const int gam = 2 * cc + g4; *(LAS f32x4*)(L + L_SL + w * 1024 + (2 * gam) * 16) = o; }
    }
    STAMP(6);
    __syncthreads();
    const unsigned tag = (epoch + 1u) & 0xffu;
    if (w == 0) {
        f32x4 s = *(LAS f32x4*)(L + L_SL + l * 16);
#pragma unroll
        for (int ww = 1; ww < 8; ++ww) s += *(LAS f32x4*)(L + L_SL + ww * 1024 + l * 16);
        __builtin_amdgcn_raw_buffer_store_b128(tag4(s, tag), rWS, WS_H2 + (head * 8 + c) * 1024 + l * 16, 0, AUX_SC1);
    }
    STAMP(7);
    v4u g;
    {
        const int off = WS_H2 + (head * 8 + (cc & 7)) * 1024 + (w * 8 + g4 * 2 + (cc >> 3)) * 16;
        for (unsigned spins = 0;; ++spins) {
            asm volatile("" ::: "memory");
            g = ld128<AUX_SC1>(rWS, off, 0);
            if (__all(tag_ok(g, tag)) || spins > SPIN_CAP) break;
            __builtin_amdgcn_s_sleep(1);
        }
    }
    STAMP(8);
    {
        f32x4 v = untag(g);
        const bool lo = cc < 8;
        const float om = dpp<ROR8>(v.x), ol = dpp<ROR8>(v.y);
        const float Mc = lo ? om : v.x, Lc = lo ? ol : v.y;
        const float M = oct_max(Mc);
        const float wt = __builtin_amdgcn_exp2f((Mc - M) * 1.4426950408889634f);
        const float Lt = oct_sum(wt * Lc);
        const float f = lo ? wt : 1.0f;
        f32x4 t;
#pragma unroll
        for (int i = 0; i < 4; ++i) t[i] = oct_sum(v[i] * f);
        const float inv = __builtin_amdgcn_rcpf(Lt);
        f32x4 a1 = {0.f, 0.f, 0.f, 0.f}, a2 = a1;
        unsigned hd[2];
#pragma unroll
        for (int i = 0; i < 2; ++i) {
            const float hh = rbf(t[2 + i]);
            const float sl = hh * __builtin_amdgcn_rcpf(1.0f + __builtin_amdgcn_exp2f(-hh * 1.4426950408889634f));
            hd[i] = cvtpk(sl, sl);
        }
#pragma unroll
        for (int i = 0; i < 4; ++i) { const float ov = t[i] * inv; a1 = vmm(cvtpk(ov, ov), mk, wo[i], a1); }
#pragma unroll
        for (int i = 0; i < 2; ++i) a2 = vmm(hd[i], mk, w2[i], a2);
        f32x4 sel = g4 < 2 ? a1 : a2, tot;
#pragma unroll
        for (int r = 0; r < 4; ++r) tot[r] = x32_sum(sel[r]);
        if (g4 < 2 && cc < 10) *(LAS f32x4*)(L + L_OS + w * 512 + (2 * cc + g4) * 16) = tot;
    }
    __syncthreads();
    STAMP(9);
    if (w == 0) {
        const int gam = l < 20 ? l : 0;
        f32x4 s = *(LAS f32x4*)(L + L_OS + gam * 16);
#pragma unroll
        for (int ww = 1; ww < 8; ++ww) s += *(LAS f32x4*)(L + L_OS + ww * 512 + gam * 16);
        if (l < 20) __builtin_amdgcn_raw_buffer_store_b128(tag4(s, tag), rWS, WS_H3 + ((c * 20 + l) * 32 + head) * 16, 0, AUX_SC1);
        STAMP(10);
        if (head < 10) {
            const int off = WS_H3 + (c * 20 + 2 * head) * 512 + l * 16;
            v4u g3;
            for (unsigned spins = 0;; ++spins) {
                asm volatile("" ::: "memory");
                g3 = ld128<AUX_SC1>(rWS, off, 0);
                if (__all(tag_ok(g3, tag)) || spins > SPIN_CAP) break;
                __builtin_amdgcn_s_sleep(1);
            }
            STAMP(11);
            f32x4 v = untag(g3);
#pragma unroll
            for (int i = 0; i < 4; ++i) v[i] = x16_sum(row16_sum(v[i]));
            v2u o;
            o.x = cvtpk(v.x + bflo(xres.x), v.y + bfhi(xres.x));
            o.y = cvtpk(v.z + bflo(xres.y), v.w + bfhi(xres.y));
            if ((l & 31) == 0) __builtin_amdgcn_raw_buffer_store_b64(o, rO, (80 * c + 8 * head + 4 * (l >> 5)) * 2, 0, 0);
            if (b == 0 && l == 0) __builtin_amdgcn_raw_buffer_store_b32(epoch + 1u, rWS, WS_EPOCH, 0, AUX_SC1);
        }
    }
    STAMP(12);
    STAMP_DUMP;
}

}

extern "C" void kernel_launch(void* const* d_in, const int* in_sizes, int n_in, void* d_out, int out_size, void* d_ws,
                              size_t ws_size, hipStream_t stream) {
    fused_decode_kernel<<<NWG, NTHR, 0, stream>>>(d_in[0], d_in[1], d_in[2], d_in[3], d_in[4], d_in[6], d_in[7], d_out, d_ws);
}
```

```cpp
#include <hip/hip_runtime.h>
#include <hip/hip_bf16.h>
#include <stdint.h>

namespace {

constexpr int D = 640, NH = 32, DH = 128, SK = 4096, WIN = 1024, FF = 1728;
constexpr int NWG = 256, NTHR = 512;

typedef unsigned v4u __attribute__((ext_vector_type(4)));
typedef unsigned v2u __attribute__((ext_vector_type(2)));
typedef float f32x4 __attribute__((ext_vector_type(4)));
typedef float f32x2 __attribute__((ext_vector_type(2)));
typedef __bf16 bf16x2_t __attribute__((ext_vector_type(2)));
typedef __bf16 bf16x8_t __attribute__((ext_vector_type(8)));
#define LAS __attribute__((address_space(3)))

constexpr int WS_EPOCH = 0;
constexpr int WS_H1 = 4096;
constexpr int WS_H2 = WS_H1 + 256 * 512;
constexpr int WS_H3 = WS_H2 + 256 * 1024;
constexpr int WS_END = WS_H3 + 8 * 32 * 384;

constexpr int L_XD = 0;
constexpr int L_S1 = 2560;
constexpr int L_QP = L_S1 + 5 * 512;
constexpr int L_MX = L_QP + 256;
constexpr int L_EP = L_MX + 32;
constexpr int L_S2 = L_MX + 64;
constexpr int L_S3 = L_S2 + 8192;
constexpr int L_END = L_S3 + 4096;
constexpr int LDS_BYTES = 84 * 1024;
static_assert(L_END <= LDS_BYTES, "LDS map");

constexpr int AUX_SC1 = 16, AUX_NT = 2;
constexpr int CODE_BYTES = 6272;
constexpr unsigned SPIN_CAP = 1u << 18;
constexpr int OOB = 0x7ff00000;

__device__ __forceinline__ __amdgpu_buffer_rsrc_t mk_rsrc(const void* p, int bytes) {
    return __builtin_amdgcn_make_buffer_rsrc((void*)p, 0, bytes, 0x00020000);
}
template <int AUX> __device__ __forceinline__ v4u ld128(__amdgpu_buffer_rsrc_t r, int voff, int soff) {
    return __builtin_amdgcn_raw_buffer_load_b128(r, voff, soff, AUX);
}
__device__ __forceinline__ float u2f(unsigned u) { return __builtin_bit_cast(float, u); }
__device__ __forceinline__ unsigned f2u(float f) { return __builtin_bit_cast(unsigned, f); }
__device__ __forceinline__ float bflo(unsigned u) { return u2f(u << 16); }
__device__ __forceinline__ float bfhi(unsigned u) { return u2f(u & 0xffff0000u); }
__device__ __forceinline__ unsigned cvtpk(float lo, float hi) {
    bf16x2_t t = {(__bf16)lo, (__bf16)hi};
    return __builtin_bit_cast(unsigned, t);
}
__device__ __forceinline__ float rbf(float x) { return bflo(cvtpk(x, x)); }
template <int CTRL> __device__ __forceinline__ float dpp(float x) {
    return u2f(__builtin_amdgcn_mov_dpp(f2u(x), CTRL, 0xf, 0xf, true));
}
constexpr int XOR1 = 0xB1, XOR2 = 0x4E, ROR8 = 0x128, ROR4 = 0x124, ROR2 = 0x122, ROR1 = 0x121;
__device__ __forceinline__ float row16_sum(float x) {
    x += dpp<ROR8>(x); x += dpp<ROR4>(x); x += dpp<ROR2>(x); x += dpp<ROR1>(x); return x;
}
#define DPP4(op, ctl) op " %0, %0, %0 " ctl " row_mask:0xf bank_mask:0xf\n\t" op " %1, %1, %1 " ctl " row_mask:0xf bank_mask:0xf\n\t" \
                      op " %2, %2, %2 " ctl " row_mask:0xf bank_mask:0xf\n\t" op " %3, %3, %3 " ctl " row_mask:0xf bank_mask:0xf\n\t"
__device__ __forceinline__ void oct_sum4(float& a, float& b, float& c, float& d) {
    asm("s_nop 1\n\t" DPP4("v_add_f32_dpp", "quad_perm:[1,0,3,2]") DPP4("v_add_f32_dpp", "quad_perm:[2,3,0,1]") DPP4("v_add_f32_dpp", "row_half_mirror") "s_nop 0"
        : "+v"(a), "+v"(b), "+v"(c), "+v"(d));
}
__device__ __forceinline__ void row16_sum4(float& a, float& b, float& c, float& d) {
    asm("s_nop 1\n\t" DPP4("v_add_f32_dpp", "row_ror:8") DPP4("v_add_f32_dpp", "row_ror:4") DPP4("v_add_f32_dpp", "row_ror:2") DPP4("v_add_f32_dpp", "row_ror:1")
        : "+v"(a), "+v"(b), "+v"(c), "+v"(d));
}
__device__ __forceinline__ float oct_max(float x) {
    float r;
    asm("s_nop 1\n\tv_max_f32_dpp %0, %1, %1 quad_perm:[1,0,3,2] row_mask:0xf bank_mask:0xf\n\ts_nop 1\n\t"
        "v_max_f32_dpp %0, %0, %0 quad_perm:[2,3,0,1] row_mask:0xf bank_mask:0xf\n\ts_nop 1\n\t"
        "v_max_f32_dpp %0, %0, %0 row_half_mirror row_mask:0xf bank_mask:0xf" : "=&v"(r) : "v"(x));
    return r;
}
__device__ __forceinline__ float max2(float a, float b) { float r; asm("v_max_f32 %0, %1, %2" : "=v"(r) : "v"(a), "v"(b)); return r; }
__device__ __forceinline__ float max3(float a, float b, float c) { float r; asm("v_max3_f32 %0, %1, %2, %3" : "=v"(r) : "v"(a), "v"(b), "v"(c)); return r; }
#define PIN(x) asm volatile("" : "+v"(x))
__device__ __forceinline__ float x16_sum(float x) {
    auto s = __builtin_amdgcn_permlane16_swap(f2u(x), f2u(x), false, false);
    return u2f(s[0]) + u2f(s[1]);
}
__device__ __forceinline__ float x32_sum(float x) {
    auto s = __builtin_amdgcn_permlane32_swap(f2u(x), f2u(x), false, false);
    return u2f(s[0]) + u2f(s[1]);
}
__device__ __forceinline__ float x16_max(float x) {
    auto s = __builtin_amdgcn_permlane16_swap(f2u(x), f2u(x), false, false);
    return max2(u2f(s[0]), u2f(s[1]));
}
__device__ __forceinline__ float x32_max(float x) {
    auto s = __builtin_amdgcn_permlane32_swap(f2u(x), f2u(x), false, false);
    return max2(u2f(s[0]), u2f(s[1]));
}
__device__ __forceinline__ float dot2(unsigned a, unsigned b, float c) {
    return __builtin_amdgcn_fdot2_f32_bf16(__builtin_bit_cast(bf16x2_t, a), __builtin_bit_cast(bf16x2_t, b), c, false);
}
__device__ __forceinline__ f32x4 vmm(unsigned dup, const unsigned (&mk)[4], v4u w, f32x4 acc) {
    v4u a = {dup & mk[0], dup & mk[1], dup & mk[2], dup & mk[3]};
    return __builtin_amdgcn_mfma_f32_16x16x32_bf16(__builtin_bit_cast(bf16x8_t, a), __builtin_bit_cast(bf16x8_t, w), acc, 0, 0, 0);
}
__device__ __forceinline__ v4u tag4(f32x4 v, unsigned tag) {
    return v4u{__builtin_amdgcn_perm(f2u(v.x), tag, 0x07060500u), __builtin_amdgcn_perm(f2u(v.y), tag, 0x07060500u),
               __builtin_amdgcn_perm(f2u(v.z), tag, 0x07060500u), __builtin_amdgcn_perm(f2u(v.w), tag, 0x07060500u)};
}
__device__ __forceinline__ v4u tag4_rn(f32x4 v, unsigned tag) {
    return tag4(f32x4{u2f(f2u(v.x) + 0x80u), u2f(f2u(v.y) + 0x80u), u2f(f2u(v.z) + 0x80u), u2f(f2u(v.w) + 0x80u)}, tag);
}
__device__ __forceinline__ bool all_tagged(v4u g, unsigned tagx4) {
    const unsigned a = __builtin_amdgcn_perm(g.y, g.x, 0x04000400u), b = __builtin_amdgcn_perm(g.w, g.z, 0x04000400u);
    return __all(__builtin_amdgcn_perm(b, a, 0x05040100u) == tagx4);
}
__device__ __forceinline__ f32x4 untag(v4u g) {
    return f32x4{u2f(g.x & 0xffffff00u), u2f(g.y & 0xffffff00u), u2f(g.z & 0xffffff00u), u2f(g.w & 0xffffff00u)};
}
__device__ __forceinline__ v4u poll(__amdgpu_buffer_rsrc_t r, int off, unsigned tagx4, v4u g) {
    if (!all_tagged(g, tagx4)) {
        for (unsigned spins = 0;; ++spins) {
            asm volatile("" ::: "memory");
            g = ld128<AUX_SC1>(r, off, 0);
            if (all_tagged(g, tagx4) || spins > SPIN_CAP) break;
        }
    }
    return g;
}

#define BAR() asm volatile("s_waitcnt lgkmcnt(0)\n\ts_barrier" ::: "memory")
#define RAWBAR() asm volatile("s_barrier" ::: "memory")
#define SB() __builtin_amdgcn_sched_barrier(0)

__global__ void __launch_bounds__(NTHR, 2)
fused_decode_kernel(const void* __restrict__ px, const void* __restrict__ pk, const void* __restrict__ pv,
                    const void* __restrict__ pwq, const void* __restrict__ pwo, const void* __restrict__ pw1,
                    const void* __restrict__ pw2, void* pout, void* pws) {
    __shared__ __attribute__((aligned(16))) unsigned char smem[LDS_BYTES];
    LAS unsigned char* L = (LAS unsigned char*)smem;
    const int b = blockIdx.x, idx = b >> 3, head = (b & 7) | (idx & ~7), c = idx & 7;
    const int tid = threadIdx.x, l = tid & 63, w = __builtin_amdgcn_readfirstlane(tid >> 6), g4 = l >> 4, cc = l & 15;
    asm volatile("" :: "s"(px), "s"(pk), "s"(pv), "s"(pwq), "s"(pwo), "s"(pw1), "s"(pw2), "s"(pout), "s"(pws));

    const __amdgpu_buffer_rsrc_t rX = mk_rsrc(px, D * 2), rK = mk_rsrc(pk, NH * SK * DH * 2), rV = mk_rsrc(pv, NH * SK * DH * 2),
                                 rWO = mk_rsrc(pwo, NH * DH * D * 2), rW2 = mk_rsrc(pw2, FF * D * 2),
                                 rWS = mk_rsrc(pws, WS_END), rO = mk_rsrc(pout, D * 2);

    v4u xa; unsigned xb, ep0, cpf = 0;
    if (w == 0) {
        xa = ld128<0>(rX, l * 20, 0);
        xb = __builtin_amdgcn_raw_buffer_load_b32(rX, l * 20 + 16, 0, 0);
        ep0 = __builtin_amdgcn_raw_buffer_load_b32(rWS, WS_EPOCH, 0, AUX_SC1);
        cpf = __builtin_amdgcn_raw_buffer_load_b32(mk_rsrc((const void*)(__builtin_amdgcn_s_getpc() & ~127ull), CODE_BYTES), (idx * 2 + (l & 1)) * 128, 0, 0);
    }
    SB();
    const bool isq = w < 5;
    v4u at[4];
    {
        const __amdgpu_buffer_rsrc_t rWQ = mk_rsrc(pwq, D * NH * DH * 2);
        int voff = g4 * (4 * NH * DH * 2) + cc * 16;
        PIN(voff);
        voff = isq ? voff : OOB;
        const int soff = (80 * c + 16 * w) * (NH * DH * 2) + head * 256;
#pragma unroll
        for (int i = 0; i < 4; ++i) at[i] = ld128<AUX_NT>(rWQ, voff, soff + i * (NH * DH * 2));
    }
    SB();
    int l16 = l * 16;
    PIN(l16);
    unsigned ptv = 0;
    if (w == 0) {
        {
            const int blk = head * 8 + c;
            const int b1 = WS_H1 + blk * 512, b2 = WS_H2 + blk * 1024 - 4 * 128, b3 = WS_H3 + (c * 32 + head) * 384 - 12 * 128;
            int a = l < 4 ? b1 : b2;
            PIN(a);
            a = (l < 12 ? a : b3) + l * 128;
            PIN(a);
            ptv = __builtin_amdgcn_raw_buffer_load_b32(rWS, l < 15 ? a : OOB, 0, AUX_SC1);
            SB();
        }
        float xv[10] = {bflo(xa.x), bfhi(xa.x), bflo(xa.y), bfhi(xa.y), bflo(xa.z), bfhi(xa.z), bflo(xa.w), bfhi(xa.w), bflo(xb), bfhi(xb)};
        float s = ((xv[0] + xv[1]) + (xv[2] + xv[3])) + ((xv[4] + xv[5]) + (xv[6] + xv[7])) + (xv[8] + xv[9]);
        s = x32_sum(x16_sum(row16_sum(s)));
        const float mean = s * (1.0f / D);
        float qa = 0.f, qb = 0.f;
#pragma unroll
        for (int i = 0; i < 5; ++i) { const float d = xv[2 * i] - mean, e = xv[2 * i + 1] - mean; qa = fmaf(d, d, qa); qb = fmaf(e, e, qb); }
        const float q2 = x32_sum(x16_sum(row16_sum(qa + qb)));
        const float var = rbf(q2 * (1.0f / D));
        const float sd = rbf(__builtin_amdgcn_sqrtf(rbf(var + 1e-5f)));
        const float rinv = __builtin_amdgcn_rcpf(sd), mb = rbf(mean);
        unsigned dup[10];
#pragma unroll
        for (int i = 0; i < 10; ++i) { const float y = rbf(xv[i] - mb) * rinv; dup[i] = cvtpk(y, y); }
        LAS v2u* xd = (LAS v2u*)(L + L_XD + l * 40);
#pragma unroll
        for (int i = 0; i < 5; ++i) xd[i] = v2u{dup[2 * i], dup[2 * i + 1]};
        if (l == 0) *(LAS unsigned*)(L + L_EP) = ep0;
    } else if (w == 1) {
        *(LAS f32x2*)(L + L_S2 + 6 * 1024 + 2 * l16 + 24) = f32x2{0.f, 0.f};
    }
    BAR();
    v4u kt[4], vt[4], wo[4], w2[2];
    SB();
    const int lrow = isq ? 16 * w + 4 * g4 : 32 * (w - 5) + 4 * (2 * g4 + (cc >> 3));
    const bool aval = lrow < 80;
    v4u bt[4];
    if (!isq) {
        int voff = __umul24(80 * c + lrow, FF * 2) + head * 128 + (cc & 7) * 16;
        PIN(voff);
        voff = (aval && head < FF / 64) ? voff : OOB;
        const __amdgpu_buffer_rsrc_t rW1 = mk_rsrc(pw1, D * FF * 2);
#pragma unroll
        for (int i = 0; i < 4; ++i) bt[i] = ld128<AUX_NT>(rW1, voff, i * (FF * 2));
    }
    SB();
    const int shoff = c * 160 + (cc < 10 ? cc : 9) * 16;
    int w2off = __umul24(head * 64 + 8 * w + 2 * g4, 1280) + shoff;
    PIN(w2off);
    SB();
#pragma unroll
    for (int i = 0; i < 2; ++i) w2[i] = ld128<AUX_NT>(rW2, w2off, i * 1280);
    SB();
    int xdrd = lrow; PIN(xdrd); xdrd = L_XD + (80 * c + (aval ? xdrd : 0)) * 4;
    const v4u dd = *(LAS v4u*)(L + xdrd);
    const unsigned epoch = __builtin_amdgcn_readfirstlane(*(LAS unsigned*)(L + L_EP));
    unsigned mk[4];
    {
        unsigned half = 0xffffu << ((cc & 1) * 16);
        const int r4 = (cc >> 1) & 3;
        PIN(half);
#pragma unroll
        for (int r = 0; r < 4; ++r) { mk[r] = r4 == r ? half : 0u; PIN(mk[r]); }
    }
    int gq16 = (2 * cc + g4) * 16;
    PIN(gq16);
    int kvoff = (head * SK + (SK - WIN) + c * 128 + w * 16 + g4) * 256 + cc * 16;
    int wooff = __umul24(head * 128 + 16 * w + 4 * g4, 1280) + shoff;
    int off1 = WS_H1 + head * 8 * 512 + w * 128 + (l & 7) * 512 + (l >> 3) * 16;
    PIN(off1); off1 = w < 4 ? off1 : OOB;
    int off2 = WS_H2 + head * 8192 + w * 128 + (cc & 7) * 1024 + (g4 * 2 + (cc >> 3)) * 16;
    int st1 = WS_H1 + (head * 8 + c) * 512 + l16; PIN(st1); st1 = l < 32 ? st1 : OOB;
    int st2 = WS_H2 + (head * 8 + c) * 1024 + l16;
    int st3 = WS_H3 + (c * 32 + head) * 384 + l16; PIN(st3); st3 = l < 24 ? st3 : OOB;
    int s1wr = L_S1 + w * 512 + gq16;
    int shwr = L_S2 + (2 * w - 10 + (g4 >> 1)) * 1024 + ((cc & 7) * 8 + (g4 & 1) * 4 + 1) * 16 + 8;
    int s2wr = L_S2 + w * 1024 + 2 * gq16;
    int s3wr = L_S3 + w * 512 + gq16;
    int cc16 = cc * 16;
    PIN(kvoff); PIN(wooff); PIN(off1); PIN(off2); PIN(st1); PIN(st2); PIN(st3); PIN(s1wr); PIN(shwr); PIN(s2wr); PIN(s3wr); PIN(cc16);
    unsigned tag = (epoch + 1u) & 0xffu;
    const unsigned tagx4 = tag * 0x01010101u;
    PIN(tag);
    SB();

    if (isq) {
        f32x4 a0 = {0.f, 0.f, 0.f, 0.f}, a1 = a0;
        a0 = vmm(dd.x, mk, at[0], a0);
        a1 = vmm(dd.y, mk, at[1], a1);
        a0 = vmm(dd.z, mk, at[2], a0);
        a1 = vmm(dd.w, mk, at[3], a1);
        a0 += a1;
        if (g4 < 2) *(LAS f32x4*)(L + s1wr) = a0;
    }
    BAR();
    if (w == 0) {
        const LAS f32x4* sp = (const LAS f32x4*)(L + L_S1 + l16);
        const f32x4 r0 = sp[0], r1 = sp[32], r2 = sp[64], r3 = sp[96], r4 = sp[128];
        SB();
        __builtin_amdgcn_raw_buffer_store_b128(tag4_rn(((r0 + r1) + (r2 + r3)) + r4, tag), rWS, st1, 0, AUX_SC1);
    }
    RAWBAR();
    SB();
#pragma unroll
    for (int t = 0; t < 4; ++t) kt[t] = ld128<AUX_NT>(rK, kvoff, t * 1024);
    SB();
    __builtin_amdgcn_s_sleep(16);
    {
        asm volatile("" ::: "memory");
        v4u g = ld128<AUX_SC1>(rWS, off1, 0);
        SB();
        RAWBAR();
#pragma unroll
        for (int i = 0; i < 4; ++i) wo[i] = ld128<AUX_NT>(rWO, wooff, i * 1280);
        SB();
        if (!isq) {
            f32x4 a0 = {0.f, 0.f, 0.f, 0.f}, a1 = a0;
            a0 = vmm(dd.x, mk, bt[0], a0);
            a1 = vmm(dd.y, mk, bt[1], a1);
            a0 = vmm(dd.z, mk, bt[2], a0);
            a1 = vmm(dd.w, mk, bt[3], a1);
            a0 += a1;
            if ((g4 >> 1) == (cc >> 3)) {
                *(LAS f32x2*)(L + shwr) = f32x2{a0.x, a0.y};
                *(LAS f32x2*)(L + shwr + 32) = f32x2{a0.z, a0.w};
            }
        }
        if (w < 4) g = poll(rWS, off1, tagx4, g);
        SB();
#pragma unroll
        for (int t = 0; t < 4; ++t) vt[t] = ld128<AUX_NT>(rV, kvoff, t * 1024);
        SB();
        asm volatile("" :: "v"(ptv), "v"(cpf), "v"(xa), "v"(xb), "v"(ep0), "v"(at[0]), "v"(at[1]), "v"(at[2]), "v"(at[3]));
        const f32x4 v = untag(g);
        float h0 = v.x, h1 = v.y, h2 = v.z, h3 = v.w;
        oct_sum4(h0, h1, h2, h3);
        if (w < 4 && (l & 7) == 0) *(LAS v2u*)(L + L_QP + w * 64 + l) = v2u{cvtpk(h0, h1), cvtpk(h2, h3)};
    }
    BAR();
    const v4u qp = *(LAS v4u*)(L + L_QP + cc16);
    float sc[4];
#pragma unroll
    for (int t = 0; t < 4; ++t) sc[t] = dot2(kt[t].w, qp.w, dot2(kt[t].z, qp.z, dot2(kt[t].y, qp.y, dot2(kt[t].x, qp.x, 0.f))));
    row16_sum4(sc[0], sc[1], sc[2], sc[3]);
#pragma unroll
    for (int t = 0; t < 4; ++t) sc[t] = rbf(rbf(sc[t]) * 0.08838834764831845f);
    float m = x32_max(x16_max(max2(max3(sc[0], sc[1], sc[2]), sc[3])));
    if (l == 0) *(LAS float*)(L + L_MX + w * 4) = m;
    BAR();
    const f32x4 ma = *(LAS f32x4*)(L + L_MX), mb4 = *(LAS f32x4*)(L + L_MX + 16);
    SB();
    m = max2(max3(max3(ma.x, ma.y, ma.z), ma.w, mb4.x), max3(mb4.y, mb4.z, mb4.w));
    unsigned pd[4];
    float pe[4];
#pragma unroll
    for (int t = 0; t < 4; ++t) { pe[t] = __builtin_amdgcn_exp2f((sc[t] - m) * 1.4426950408889634f); pd[t] = cvtpk(pe[t], pe[t]); }
    const float lsum = x32_sum(x16_sum((pe[0] + pe[1]) + (pe[2] + pe[3])));
    if (l < 32) *(LAS f32x2*)(L + L_S2 + w * 1024 + 16 + 2 * l16) = f32x2{w == 0 ? m : 0.f, lsum};
    v4u pa[4];
#pragma unroll
    for (int t = 0; t < 4; ++t) { pa[t] = v4u{pd[t] & mk[0], pd[t] & mk[1], pd[t] & mk[2], pd[t] & mk[3]}; PIN(pa[t]); }
    SB();
    {
        f32x4 o = {0.f, 0.f, 0.f, 0.f};
#pragma unroll
        for (int t = 0; t < 4; ++t)
            o = __builtin_amdgcn_mfma_f32_16x16x32_bf16(__builtin_bit_cast(bf16x8_t, pa[t]), __builtin_bit_cast(bf16x8_t, vt[t]), o, 0, 0, 0);
        if (g4 < 2) *(LAS f32x4*)(L + s2wr) = o;
    }
    BAR();
    if (w == 0) {
        const LAS f32x4* sp = (const LAS f32x4*)(L + L_S2 + l16);
        const f32x4 r0 = sp[0], r1 = sp[64], r2 = sp[128], r3 = sp[192], r4 = sp[256], r5 = sp[320], r6 = sp[384], r7 = sp[448];
        SB();
        __builtin_amdgcn_raw_buffer_store_b128(tag4(((r0 + r1) + (r2 + r3)) + ((r4 + r5) + (r6 + r7)), tag), rWS, st2, 0, AUX_SC1);
    }
    {
        asm volatile("" ::: "memory");
        v4u g = ld128<AUX_SC1>(rWS, off2, 0);
        g = poll(rWS, off2, tagx4, g);
        const f32x4 v = untag(g);
        const float M = oct_max(v.x);
        const float wt = __builtin_amdgcn_exp2f((v.x - M) * 1.4426950408889634f);
        const float wl = dpp<ROR8>(wt);
        const float f = cc < 8 ? wl : 1.0f;
        float o0 = cc < 8 ? v.x * wl : wt * v.y, o1 = v.y * wl, o2 = v.z * f, o3 = v.w * f;
        oct_sum4(o0, o1, o2, o3);
        const float inv = __builtin_amdgcn_rcpf(u2f(__builtin_amdgcn_readlane(f2u(o0), 8)));
        const float ha = rbf(dpp<ROR8>(o2)), hb = rbf(dpp<ROR8>(o3));
        const float sa = ha * __builtin_amdgcn_rcpf(1.0f + __builtin_amdgcn_exp2f(-ha * 1.4426950408889634f));
        const float sb = hb * __builtin_amdgcn_rcpf(1.0f + __builtin_amdgcn_exp2f(-hb * 1.4426950408889634f));
        f32x4 a1 = {0.f, 0.f, 0.f, 0.f}, a3 = a1, a2 = a1;
        a1 = vmm(cvtpk(o0, o0), mk, wo[0], a1);
        a3 = vmm(cvtpk(o1, o1), mk, wo[1], a3);
        a1 = vmm(cvtpk(o2, o2), mk, wo[2], a1);
        a3 = vmm(cvtpk(o3, o3), mk, wo[3], a3);
        a2 = vmm(cvtpk(sa, sa), mk, w2[0], a2);
        a2 = vmm(cvtpk(sb, sb), mk, w2[1], a2);
        a1 = (a1 + a3) * inv + a2;
        if (g4 < 2 && cc < 10) *(LAS f32x4*)(L + s3wr) = a1;
    }
    BAR();
    if (w == 0) {
        const LAS f32x4* sp = (const LAS f32x4*)(L + L_S3 + l16);
        const f32x4 r0 = sp[0], r1 = sp[32], r2 = sp[64], r3 = sp[96], r4 = sp[128], r5 = sp[160], r6 = sp[192], r7 = sp[224];
        SB();
        __builtin_amdgcn_raw_buffer_store_b128(tag4(((r0 + r1) + (r2 + r3)) + ((r4 + r5) + (r6 + r7)), tag), rWS, st3, 0, AUX_SC1);
    }
    if (w == 1 && head < 10) {
        const int off3 = WS_H3 + (c * 32 + (l & 31)) * 384 + (2 * head + (l >> 5)) * 16;
        const int ocol = (80 * c + 8 * head + 4 * (l >> 5)) * 2;
        const unsigned xr = __builtin_amdgcn_raw_buffer_load_b16(rX, ocol + (l & 3) * 2, 0, 0);
        asm volatile("" ::: "memory");
        v4u g = ld128<AUX_SC1>(rWS, off3, 0);
        g = poll(rWS, off3, tagx4, g);
        const f32x4 v = untag(g);
        const bool b0 = l & 1, b1 = l & 2;
        const float p0 = (b0 ? v.y : v.x) + dpp<XOR1>(b0 ? v.x : v.y);
        const float p1 = (b0 ? v.w : v.z) + dpp<XOR1>(b0 ? v.z : v.w);
        float t = (b1 ? p1 : p0) + dpp<XOR2>(b1 ? p0 : p1);
        t += dpp<ROR4>(t); t += dpp<ROR8>(t);
        t = x16_sum(t);
        const float y = t + bflo(xr);
        const unsigned pr = cvtpk(y, dpp<XOR1>(y));
        const v2u o4 = {pr, (unsigned)__builtin_amdgcn_mov_dpp(pr, XOR2, 0xf, 0xf, true)};
        __builtin_amdgcn_raw_buffer_store_b64(o4, rO, (l & 31) == 0 ? ocol : OOB, 0, AUX_SC1);
        if (b == 0 && l == 0) __builtin_amdgcn_raw_buffer_store_b32(epoch + 1u, rWS, WS_EPOCH, 0, AUX_SC1);
    }
}

}

extern "C" void kernel_launch(void* const* d_in, const int* in_sizes, int n_in, void* d_out, int out_size, void* d_ws,
                              size_t ws_size, hipStream_t stream) {
    fused_decode_kernel<<<NWG, NTHR, 0, stream>>>(d_in[0], d_in[1], d_in[2], d_in[3], d_in[4], d_in[6], d_in[7], d_out, d_ws);
}
```
